```python
import torch

def _softmax(x, axis=-1):
    return torch.softmax(x, dim=axis)
def _gelu(x):
    return torch.nn.functional.gelu(x, approximate="tanh")
def _silu(x):
    return torch.nn.functional.silu(x)
def _sigmoid(x):
    return torch.sigmoid(x)
def _layernorm(x, axis=-1, eps=1e-5):
    m = x.mean(dim=axis, keepdim=True)
    v = x.var(dim=axis, keepdim=True, unbiased=False)
    return (x - m) / torch.sqrt(v + eps)
def _scatter_add(tgt, idx, src, axis=0):
    return tgt.clone().index_add_(axis, idx, src)
def _segment_sum(src, seg, n):
    out = torch.zeros((n,) + src.shape[1:], dtype=src.dtype)
    return out.index_add_(0, seg, src)
def _pad(x, pw):
    flat = tuple(v for pair in reversed(pw) for v in pair)
    return torch.nn.functional.pad(x, flat)

kernel_name = 'standalone__softmax_full_bidir__sampling'

def setup_inputs():

    _w = (lambda a: (lambda t: t.to(torch.bfloat16) if t.is_floating_point() else t)(torch.as_tensor(a)))
    B, H, Sq, Skv, Dh = 8, 32, 1, 4096, 128
    return {
        "q": _w((lambda *s: torch.randn(*s, dtype=torch.bfloat16) * 0.8)(B, H, Sq, Dh)),
        "k": _w((lambda *s: torch.randn(*s, dtype=torch.bfloat16) * 0.8)(B, H, Skv, Dh)),
        "v": _w((lambda *s: torch.randn(*s, dtype=torch.bfloat16) * 0.05)(B, H, Skv, Dh)),
    }

def reference(q, k, v):
    scale = q.shape[-1] ** -0.5
    scores = torch.einsum('bhqd,bhkd->bhqk', q, k) * scale
    attn = _softmax(scores, axis=-1)
    return torch.einsum('bhqk,bhkd->bhqd', attn, v)
```

```cpp
#include <hip/hip_runtime.h>
#include <hip/hip_bf16.h>
#include <stdint.h>

constexpr int D = 128, S = 4096;
constexpr int ROWB = D * 2;

using bf16x2 = __attribute__((ext_vector_type(2))) __bf16;
using u32x4 = __attribute__((ext_vector_type(4))) unsigned;
using f32x4 = __attribute__((ext_vector_type(4))) float;

__device__ __forceinline__ float dot2(unsigned k, unsigned q, float acc) {
  return __builtin_amdgcn_fdot2_f32_bf16(__builtin_bit_cast(bf16x2, k), __builtin_bit_cast(bf16x2, q), acc, false);
}
template <int CTRL> __device__ __forceinline__ float dpp(float x) {
  return __builtin_bit_cast(float, __builtin_amdgcn_mov_dpp(__builtin_bit_cast(int, x), CTRL, 0xf, 0xf, true));
}
constexpr int XOR1 = 0xB1, XOR2 = 0x4E, XOR7 = 0x141, XOR8 = 0x128;
__device__ __forceinline__ float xrow_max(float x) {
  auto s = __builtin_amdgcn_permlane16_swap(__float_as_uint(x), __float_as_uint(x), false, false);
  x = fmaxf(__uint_as_float(s[0]), __uint_as_float(s[1]));
  auto t = __builtin_amdgcn_permlane32_swap(__float_as_uint(x), __float_as_uint(x), false, false);
  return fmaxf(__uint_as_float(t[0]), __uint_as_float(t[1]));
}
__device__ __forceinline__ float xrow_sum(float x) {
  auto s = __builtin_amdgcn_permlane16_swap(__float_as_uint(x), __float_as_uint(x), false, false);
  x = __uint_as_float(s[0]) + __uint_as_float(s[1]);
  auto t = __builtin_amdgcn_permlane32_swap(__float_as_uint(x), __float_as_uint(x), false, false);
  return __uint_as_float(t[0]) + __uint_as_float(t[1]);
}
__device__ __forceinline__ float row_max(float x) {
  x = fmaxf(x, dpp<XOR8>(x)); x = fmaxf(x, dpp<XOR7>(x)); x = fmaxf(x, dpp<XOR2>(x)); return fmaxf(x, dpp<XOR1>(x));
}
__device__ __forceinline__ float row_sum(float x) {
  x += dpp<XOR8>(x); x += dpp<XOR7>(x); x += dpp<XOR2>(x); return x + dpp<XOR1>(x);
}
template <int AUX> __device__ __forceinline__ u32x4 ldb(__amdgpu_buffer_rsrc_t rs, unsigned voff, unsigned soff) {
  return __builtin_bit_cast(u32x4, __builtin_amdgcn_raw_buffer_load_b128(rs, voff, soff, AUX));
}

template <int U> struct Tile { u32x4 x[U]; };
template <int U, int AUX> __device__ __forceinline__ void issue(Tile<U>& t, __amdgpu_buffer_rsrc_t rs, unsigned voff, unsigned soff) {
#pragma unroll
  for (int j = 0; j < U; ++j) t.x[j] = ldb<AUX>(rs, voff + (j & 3) * 1024, soff + (j >> 2) * 4096);
}

template <int U> __device__ __forceinline__ void scores(const Tile<U>& t, const u32x4& qv, int c, float* sc, float& mx) {
  constexpr float C2 = 0.088388347648318440f * 1.4426950408889634f;
#pragma unroll
  for (int g = 0; g < U; g += 4) {
    float x[4];
#pragma unroll
    for (int j = 0; j < 4; ++j) {
      float d = 0.f;
#pragma unroll
      for (int e = 0; e < 4; ++e) d = dot2(t.x[g + j][e], qv[e], d);
      x[j] = d;
    }
    const bool h8 = c & 8, h4 = c & 4;
    const float a0 = h8 ? x[2] : x[0], b0 = h8 ? x[0] : x[2], a1 = h8 ? x[3] : x[1], b1 = h8 ? x[1] : x[3];
    const float y0 = a0 + dpp<XOR8>(b0), y1 = a1 + dpp<XOR8>(b1);
    const float a = h4 ? y1 : y0, b = h4 ? y0 : y1;
    float z = a + dpp<XOR7>(b);
    z += dpp<XOR2>(z);
    z += dpp<XOR1>(z);
    z *= C2;
    mx = fmaxf(mx, z);
    if ((c & 3) == 0) sc[g + (c >> 2)] = z;
  }
}

template <int U> __device__ __forceinline__ void pv(const Tile<U>& t, const float* pp, float (&acc)[8]) {
#pragma unroll
  for (int g = 0; g < U; g += 4) {
    const f32x4 p = *reinterpret_cast<const f32x4*>(pp + g);
#pragma unroll
    for (int j = 0; j < 4; ++j)
#pragma unroll
      for (int e = 0; e < 4; ++e) {
        const unsigned w = t.x[g + j][e];
        acc[2 * e] = fmaf(p[j], __builtin_bit_cast(float, w << 16), acc[2 * e]);
        acc[2 * e + 1] = fmaf(p[j], __builtin_bit_cast(float, w & 0xffff0000u), acc[2 * e + 1]);
      }
  }
}

constexpr int PACE_DT = 136, PACE_LEAD = 1670, PACE_STAGGER = 34;
__device__ __forceinline__ unsigned ticks() { return (unsigned)__builtin_amdgcn_s_memrealtime(); }
__device__ __forceinline__ void pace(unsigned now, unsigned slot) {
  int rem;
  asm volatile("s_sub_i32 %0, %1, %2\n"
               "1:\n"
               "s_cmp_lt_i32 %0, 1\n"
               "s_cbranch_scc1 2f\n"
               "s_sleep 1\n"
               "s_sub_i32 %0, %0, 3\n"
               "s_branch 1b\n"
               "2:"
               : "=&s"(rem) : "s"(slot), "s"(now) : "scc", "memory");
}

constexpr int NW = 4, U = 8, AUX_NT = 2;

__global__ void __launch_bounds__(NW * 64)
attn_decode_kernel(const __hip_bfloat16* __restrict__ q, const __hip_bfloat16* __restrict__ k,
                   const __hip_bfloat16* __restrict__ v, __hip_bfloat16* __restrict__ out) {
  constexpr int TILE_B = U * 1024, ROUND_B = NW * TILE_B, NR = S * ROWB / ROUND_B;
  constexpr int TPT = 4 * U, TPW = S / NW;
  static_assert(U % 4 == 0 && TPW % 256 == 0, "");
  __shared__ __attribute__((aligned(16))) float sm_s[S];
  __shared__ __attribute__((aligned(16))) float sm_acc[NW][D];
  __shared__ float sm_m[NW], sm_l[NW];
  const int bh = blockIdx.x;
  const int tid = threadIdx.x, lane = tid & 63;
  const int wave = __builtin_amdgcn_readfirstlane(tid >> 6);
  const int r = lane >> 4, c = lane & 15;
  const u32x4 qv = *reinterpret_cast<const u32x4*>(reinterpret_cast<const char*>(q) + (size_t)bh * ROWB + c * 16);
  const __amdgpu_buffer_rsrc_t kb = __builtin_amdgcn_make_buffer_rsrc((void*)(reinterpret_cast<const char*>(k) + (size_t)bh * S * ROWB), 0, S * ROWB, 0x00020000);
  const __amdgpu_buffer_rsrc_t vb = __builtin_amdgcn_make_buffer_rsrc((void*)(reinterpret_cast<const char*>(v) + (size_t)bh * S * ROWB), 0, S * ROWB, 0x00020000);
  const unsigned voff = wave * TILE_B + lane * 16;
  float* const sw = sm_s + wave * TPW;
  Tile<U> t;
  issue<U, AUX_NT>(t, kb, voff, 0);
  __builtin_amdgcn_sched_barrier(0);
  unsigned slot = ticks() + NR * PACE_DT - PACE_LEAD + wave * PACE_STAGGER;

  float mx = -INFINITY;
  for (int i = 0; i < NR - 1; ++i) {
    scores<U>(t, qv, c, sw + i * TPT + r * U, mx);
    __builtin_amdgcn_sched_barrier(0);
    issue<U, AUX_NT>(t, kb, voff, (i + 1) * ROUND_B);
    __builtin_amdgcn_sched_barrier(0);
  }
  scores<U>(t, qv, c, sw + (NR - 1) * TPT + r * U, mx);
  __builtin_amdgcn_sched_barrier(0);
  issue<U, AUX_NT>(t, vb, voff, 0);
  __builtin_amdgcn_sched_barrier(0);
  const float m = xrow_max(row_max(mx));
  float l = 0.f;
#pragma unroll
  for (int i = 0; i < TPW / 256; ++i) {
    f32x4 x = *reinterpret_cast<const f32x4*>(sw + i * 256 + lane * 4);
#pragma unroll
    for (int e = 0; e < 4; ++e) { x[e] = __builtin_amdgcn_exp2f(x[e] - m); l += x[e]; }
    *reinterpret_cast<f32x4*>(sw + i * 256 + lane * 4) = x;
  }
  l = xrow_sum(row_sum(l));

  float acc[8];
#pragma unroll
  for (int e = 0; e < 8; ++e) acc[e] = 0.f;
  for (int i = 0; i < NR - 1; ++i) {
    const unsigned now = ticks();
    __builtin_amdgcn_sched_barrier(0);
    pv<U>(t, sw + i * TPT + r * U, acc);
    __builtin_amdgcn_sched_barrier(0);
    slot += PACE_DT;
    pace(now, slot);
    issue<U, AUX_NT>(t, vb, voff, (i + 1) * ROUND_B);
    __builtin_amdgcn_sched_barrier(0);
  }
  pv<U>(t, sw + (NR - 1) * TPT + r * U, acc);

#pragma unroll
  for (int e = 0; e < 8; ++e) acc[e] = xrow_sum(acc[e]);
  if (r == 0) {
    *reinterpret_cast<f32x4*>(&sm_acc[wave][8 * c]) = f32x4{acc[0], acc[1], acc[2], acc[3]};
    *reinterpret_cast<f32x4*>(&sm_acc[wave][8 * c + 4]) = f32x4{acc[4], acc[5], acc[6], acc[7]};
    if (c == 0) { sm_m[wave] = m; sm_l[wave] = l; }
  }
  __syncthreads();
  if (tid < D) {
    float M = sm_m[0];
#pragma unroll
    for (int i = 1; i < NW; ++i) M = fmaxf(M, sm_m[i]);
    float L = 0.f, o = 0.f;
#pragma unroll
    for (int i = 0; i < NW; ++i) {
      const float w = __builtin_amdgcn_exp2f(sm_m[i] - M);
      L = fmaf(w, sm_l[i], L);
      o = fmaf(w, sm_acc[i][tid], o);
    }
    __hip_atomic_store(reinterpret_cast<unsigned short*>(out) + (size_t)bh * D + tid,
                       __builtin_bit_cast(unsigned short, __float2bfloat16(o / L)), __ATOMIC_RELAXED, __HIP_MEMORY_SCOPE_AGENT);
  }
}

extern "C" void kernel_launch(void* const* d_in, const int* in_sizes, int n_in, void* d_out, int out_size,
                              void* d_ws, size_t ws_size, hipStream_t stream) {
  const int BH = in_sizes[0] / D;
  attn_decode_kernel<<<BH, NW * 64, 0, stream>>>(
      (const __hip_bfloat16*)d_in[0], (const __hip_bfloat16*)d_in[1], (const __hip_bfloat16*)d_in[2], (__hip_bfloat16*)d_out);
}
```
